```python
import torch

def _softmax(x, axis=-1):
    return torch.softmax(x, dim=axis)
def _gelu(x):
    return torch.nn.functional.gelu(x, approximate="tanh")
def _silu(x):
    return torch.nn.functional.silu(x)
def _sigmoid(x):
    return torch.sigmoid(x)
def _layernorm(x, axis=-1, eps=1e-5):
    m = x.mean(dim=axis, keepdim=True)
    v = x.var(dim=axis, keepdim=True, unbiased=False)
    return (x - m) / torch.sqrt(v + eps)
def _scatter_add(tgt, idx, src, axis=0):
    return tgt.clone().index_add_(axis, idx, src)
def _segment_sum(src, seg, n):
    out = torch.zeros((n,) + src.shape[1:], dtype=src.dtype)
    return out.index_add_(0, seg, src)
def _pad(x, pw):
    flat = tuple(v for pair in reversed(pw) for v in pair)
    return torch.nn.functional.pad(x, flat)

kernel_name = 'parallel_sum__mha_block_cached__glu_gated_bilinear__decode'

def setup_inputs():

    _w = (lambda a: (lambda t: t.to(torch.bfloat16) if t.is_floating_point() else t)(torch.as_tensor(a)))
    def _A_setup():
        B, Sq, D = 1, 1, 576
        H, Sk, Dh = 32, 16384, 128
        return {
            "x": _w((lambda *s: torch.randn(*s, dtype=torch.bfloat16) * 0.05)(B, Sq, D)),
            "k_cache": _w((lambda *s: torch.randn(*s, dtype=torch.bfloat16) * 3.2)(B, H, Sk, Dh)),
            "v_cache": _w((lambda *s: torch.randn(*s, dtype=torch.bfloat16) * 1.6)(B, H, Sk, Dh)),
            "wq": _w((lambda *s: torch.randn(*s, dtype=torch.bfloat16) * 3.2)(D, H * Dh) * (D ** -0.5)),
            "wo": _w((lambda *s: torch.randn(*s, dtype=torch.bfloat16) * 1.6)(H * Dh, D) * ((H * Dh) ** -0.5)),
        }
    _A_in = _A_setup()

    def _B_setup():
        B, S, D, F = 1, 1, 576, 4608
        return {
            "x": _w((lambda *s: torch.randn(*s, dtype=torch.bfloat16) * 0.05)(B, S, D)),
            "w_gate": _w((lambda *s: torch.randn(*s, dtype=torch.bfloat16) * 1.6)(D, F) * (D ** -0.5)),
            "w_up": _w((lambda *s: torch.randn(*s, dtype=torch.bfloat16) * 1.6)(D, F) * (D ** -0.5)),
            "w_down": _w((lambda *s: torch.randn(*s, dtype=torch.bfloat16) * 1.6)(F, D) * (F ** -0.5)),
        }
    _B_in = _B_setup()

    return {
        "A_x": _A_in["x"],
        "A_k_cache": _A_in["k_cache"],
        "A_v_cache": _A_in["v_cache"],
        "A_wq": _A_in["wq"],
        "A_wo": _A_in["wo"],
        "B_x": _B_in["x"],
        "B_w_gate": _B_in["w_gate"],
        "B_w_up": _B_in["w_up"],
        "B_w_down": _B_in["w_down"],
    }

def reference(A_x, A_k_cache, A_v_cache, A_wq, A_wo, B_x, B_w_gate, B_w_up, B_w_down):
    def _A_ref(x, k_cache, v_cache, wq, wo):
        B, Sq, D = x.shape
        H, Dh = 32, 128
        q = (lambda x, *d: x.permute(*d))((x @ wq).reshape(B, Sq, H, Dh), 0, 2, 1, 3)
        scores = torch.einsum("bhqd,bhkd->bhqk", q, k_cache) * (Dh ** -0.5)
        p = _softmax(scores, axis=-1)
        o = torch.einsum("bhqk,bhkd->bhqd", p, v_cache)
        return (lambda x, *d: x.permute(*d))(o, 0, 2, 1, 3).reshape(B, Sq, H * Dh) @ wo

    def _B_ref(x, w_gate, w_up, w_down):
        g = torch.einsum("bsd,df->bsf", x, w_gate)
        u = torch.einsum("bsd,df->bsf", x, w_up)
        h = g * u
        return torch.einsum("bsf,fd->bsd", h, w_down)

    x = A_x
    xn = _layernorm(x, axis=-1)
    return x + _A_ref(xn, A_k_cache, A_v_cache, A_wq, A_wo) + _B_ref(xn, B_w_gate, B_w_up, B_w_down)
```

```cpp
#include <hip/hip_runtime.h>
#include <hip/hip_bf16.h>
#include <cstdint>

#define MIMIC 0
#define KV_NT 1

typedef unsigned u32x4 __attribute__((ext_vector_type(4)));
typedef float f32x4 __attribute__((ext_vector_type(4)));
typedef __bf16 bf16x2 __attribute__((ext_vector_type(2)));

constexpr int D = 576, H = 32, SK = 16384, DH = 128, F = 4608;
constexpr int NSPLIT = 8, KEYS = SK / NSPLIT;
constexpr int AW = 8;

__device__ __forceinline__ float bf2f(unsigned short b) { return __uint_as_float((unsigned)b << 16); }
__device__ __forceinline__ float rbf(float x) {
  return __bfloat162float(__float2bfloat16(x));
}
__device__ __forceinline__ float mr(float x) { return MIMIC ? rbf(x) : x; }
__device__ __forceinline__ float lo16(unsigned u) { return __uint_as_float(u << 16); }
__device__ __forceinline__ float hi16(unsigned u) { return __uint_as_float(u & 0xffff0000u); }
__device__ __forceinline__ float dot2(unsigned k, unsigned q, float acc) {
  return __builtin_amdgcn_fdot2_f32_bf16(__builtin_bit_cast(bf16x2, k), __builtin_bit_cast(bf16x2, q), acc, false);
}
template <int CTRL> __device__ __forceinline__ float dpp(float x) {
  return __builtin_bit_cast(float, __builtin_amdgcn_mov_dpp(__builtin_bit_cast(int, x), CTRL, 0xf, 0xf, true));
}
constexpr int XOR1 = 0xB1, XOR2 = 0x4E, XOR7 = 0x141, XOR8 = 0x128;
__device__ __forceinline__ float row_sum16(float x) {
  x += dpp<XOR1>(x); x += dpp<XOR2>(x); x += dpp<XOR7>(x); x += dpp<XOR8>(x); return x;
}
__device__ __forceinline__ u32x4 ldnt(const void* p) {
  return KV_NT ? __builtin_nontemporal_load(reinterpret_cast<const u32x4*>(p)) : *reinterpret_cast<const u32x4*>(p);
}

constexpr size_t WS_Q = 0;
constexpr size_t WS_H = 4096;
constexpr size_t WS_PML = WS_H + F;
constexpr size_t WS_PO = WS_PML + 512;
constexpr size_t WS_Y = WS_PO + 256 * 128;

__device__ __forceinline__ void layernorm_lds(const unsigned short* x, float* xn, int tid, int nthr) {
  const int lane = tid & 63;
  float v[9]; float s = 0.f;
#pragma unroll
  for (int i = 0; i < 9; ++i) { v[i] = bf2f(x[lane + 64 * i]); s += v[i]; }
#pragma unroll
  for (int o = 32; o > 0; o >>= 1) s += __shfl_xor(s, o);
  const float mean = s * (1.f / D);
  float q = 0.f;
#pragma unroll
  for (int i = 0; i < 9; ++i) { const float d = v[i] - mean; q += d * d; }
#pragma unroll
  for (int o = 32; o > 0; o >>= 1) q += __shfl_xor(q, o);
  const float var = q * (1.f / D);
  if (tid < 64) {
#pragma unroll
    for (int i = 0; i < 9; ++i) {
      float r;
      if (MIMIC) { const float m = rbf(mean), vv = rbf(var); const float den = rbf(sqrtf(rbf(vv + 1e-5f))); r = rbf(rbf(v[i] - m) / den); }
      else r = (v[i] - mean) / sqrtf(var + 1e-5f);
      xn[lane + 64 * i] = r;
    }
  }
  __syncthreads();
}

__global__ void __launch_bounds__(256) k_pre(const unsigned short* x, const unsigned short* wq, const unsigned short* wg, const unsigned short* wu, float* ws) {
  __shared__ float xn[D];
  __shared__ float red[2][16][128];
  const int tid = threadIdx.x, r = tid >> 4, c = tid & 15, b = blockIdx.x;
  layernorm_lds(x, xn, tid, 256);
  const bool isq = b < 32;
  const int nm = isq ? 1 : 2;
  for (int m = 0; m < nm; ++m) {
    const unsigned short* W = isq ? wq + b * 128 : (m == 0 ? wg : wu) + (b - 32) * 128;
    const int ld = isq ? 4096 : F;
    float acc[8] = {0, 0, 0, 0, 0, 0, 0, 0};
#pragma unroll 6
    for (int i = 0; i < 36; ++i) {
      const int k = r + 16 * i;
      const u32x4 w = *reinterpret_cast<const u32x4*>(W + (size_t)k * ld + 8 * c);
      const float xv = xn[k];
#pragma unroll
      for (int j = 0; j < 4; ++j) { acc[2 * j] = fmaf(xv, lo16(w[j]), acc[2 * j]); acc[2 * j + 1] = fmaf(xv, hi16(w[j]), acc[2 * j + 1]); }
    }
#pragma unroll
    for (int j = 0; j < 8; ++j) red[m][r][8 * c + j] = acc[j];
  }
  __syncthreads();
  if (tid < 128) {
    float s0 = 0.f, s1 = 0.f;
    for (int i = 0; i < 16; ++i) { s0 += red[0][i][tid]; if (!isq) s1 += red[1][i][tid]; }
    if (isq) reinterpret_cast<__hip_bfloat16*>(ws + WS_Q)[b * 128 + tid] = __float2bfloat16(s0);
    else ws[WS_H + (b - 32) * 128 + tid] = mr(mr(s0) * mr(s1));
  }
}

__global__ void __launch_bounds__(AW * 64, 2) k_attn(const unsigned short* K, const unsigned short* V, float* ws) {
  __shared__ float mrg[AW][4][132];
  const int tid = threadIdx.x, lane = tid & 63, wave = __builtin_amdgcn_readfirstlane(tid >> 6), r = lane >> 4, c = lane & 15;
  const int b = blockIdx.x, h = (b & 7) + 8 * (b >> 6), s = (b >> 3) & 7;
  const u32x4 q = *reinterpret_cast<const u32x4*>(reinterpret_cast<const unsigned short*>(ws + WS_Q) + h * 128 + 8 * c);
  constexpr int TPW = KEYS / AW / 16;
  const size_t base = ((size_t)h * SK + (size_t)s * KEYS) * DH + (size_t)wave * 16 * DH + (size_t)lane * 8;
  const unsigned short* kp = K + base;
  const unsigned short* vp = V + base;
  constexpr float SC = 0.088388347648318440f, L2E = 1.4426950408889634f;
  u32x4 kb[2][4], vb[2][4];
  float m = -INFINITY, l = 0.f, acc[8] = {0, 0, 0, 0, 0, 0, 0, 0};
#pragma unroll
  for (int u = 0; u < 2; ++u) {
#pragma unroll
    for (int t = 0; t < 4; ++t) kb[u][t] = ldnt(kp + (size_t)u * AW * 16 * DH + t * 4 * DH);
#pragma unroll
    for (int t = 0; t < 4; ++t) vb[u][t] = ldnt(vp + (size_t)u * AW * 16 * DH + t * 4 * DH);
  }
  for (int i = 0; i < TPW; i += 2) {
#pragma unroll
    for (int u = 0; u < 2; ++u) {
      float sc[4];
#pragma unroll
      for (int t = 0; t < 4; ++t) {
        float d = 0.f;
#pragma unroll
        for (int j = 0; j < 4; ++j) d = dot2(kb[u][t][j], q[j], d);
        d = row_sum16(d);
        sc[t] = mr(mr(d) * SC) * L2E;
      }
      if (i + u + 2 < TPW) {
#pragma unroll
        for (int t = 0; t < 4; ++t) kb[u][t] = ldnt(kp + (size_t)(i + u + 2) * AW * 16 * DH + t * 4 * DH);
      }
      const float mn = fmaxf(fmaxf(fmaxf(sc[0], sc[1]), fmaxf(sc[2], sc[3])), m);
      const float al = __builtin_amdgcn_exp2f(m - mn);
      m = mn;
      float p[4];
#pragma unroll
      for (int t = 0; t < 4; ++t) p[t] = __builtin_amdgcn_exp2f(sc[t] - mn);
      l = l * al + ((p[0] + p[1]) + (p[2] + p[3]));
#pragma unroll
      for (int j = 0; j < 8; ++j) acc[j] *= al;
#pragma unroll
      for (int t = 0; t < 4; ++t) {
#pragma unroll
        for (int j = 0; j < 4; ++j) { acc[2 * j] = fmaf(p[t], lo16(vb[u][t][j]), acc[2 * j]); acc[2 * j + 1] = fmaf(p[t], hi16(vb[u][t][j]), acc[2 * j + 1]); }
      }
      if (i + u + 2 < TPW) {
#pragma unroll
        for (int t = 0; t < 4; ++t) vb[u][t] = ldnt(vp + (size_t)(i + u + 2) * AW * 16 * DH + t * 4 * DH);
      }
    }
  }
#pragma unroll
  for (int j = 0; j < 8; ++j) mrg[wave][r][4 + 8 * c + j] = acc[j];
  if (c == 0) { mrg[wave][r][0] = m; mrg[wave][r][1] = l; }
  __syncthreads();
  if (tid < 128) {
    float M = -INFINITY;
    for (int w = 0; w < AW * 4; ++w) M = fmaxf(M, mrg[w >> 2][w & 3][0]);
    float L = 0.f, o = 0.f;
    for (int w = 0; w < AW * 4; ++w) { const float e = __builtin_amdgcn_exp2f(mrg[w >> 2][w & 3][0] - M); L = fmaf(e, mrg[w >> 2][w & 3][1], L); o = fmaf(e, mrg[w >> 2][w & 3][4 + tid], o); }
    const int slot = h * NSPLIT + s;
    ws[WS_PO + slot * 128 + tid] = o;
    if (tid == 0) { ws[WS_PML + 2 * slot] = M; ws[WS_PML + 2 * slot + 1] = L; }
  }
}

__global__ void __launch_bounds__(576) k_post(const unsigned short* wo, const unsigned short* wd, float* ws) {
  __shared__ float in[128];
  __shared__ float red[8][576];
  const int tid = threadIdx.x, b = blockIdx.x;
  const int r = tid / 72, c = tid % 72;
  const unsigned short* W = b < 32 ? wo + (size_t)b * 128 * D : wd + (size_t)(b - 32) * 128 * D;
  u32x4 w[16];
#pragma unroll
  for (int i = 0; i < 16; ++i) w[i] = *reinterpret_cast<const u32x4*>(W + (size_t)(r + 8 * i) * D + 8 * c);
  if (tid < 128) {
    if (b < 32) {
      float M = -INFINITY;
      for (int s = 0; s < NSPLIT; ++s) M = fmaxf(M, ws[WS_PML + 2 * (b * NSPLIT + s)]);
      float L = 0.f, o = 0.f;
      for (int s = 0; s < NSPLIT; ++s) { const float e = __builtin_amdgcn_exp2f(ws[WS_PML + 2 * (b * NSPLIT + s)] - M); L = fmaf(e, ws[WS_PML + 2 * (b * NSPLIT + s) + 1], L); o = fmaf(e, ws[WS_PO + (b * NSPLIT + s) * 128 + tid], o); }
      in[tid] = mr(o / L);
    } else in[tid] = ws[WS_H + (b - 32) * 128 + tid];
  }
  __syncthreads();
  float acc[8] = {0, 0, 0, 0, 0, 0, 0, 0};
#pragma unroll
  for (int i = 0; i < 16; ++i) {
    const float xv = in[r + 8 * i];
#pragma unroll
    for (int j = 0; j < 4; ++j) { acc[2 * j] = fmaf(xv, lo16(w[i][j]), acc[2 * j]); acc[2 * j + 1] = fmaf(xv, hi16(w[i][j]), acc[2 * j + 1]); }
  }
#pragma unroll
  for (int j = 0; j < 8; ++j) red[r][8 * c + j] = acc[j];
  __syncthreads();
  float s = 0.f;
#pragma unroll
  for (int i = 0; i < 8; ++i) s += red[i][tid];
  ws[WS_Y + b * D + tid] = s;
}

__global__ void __launch_bounds__(64) k_fin(const unsigned short* x, const float* ws, __hip_bfloat16* out) {
  const int j = blockIdx.x * 64 + threadIdx.x;
  float a = 0.f, bb = 0.f;
#pragma unroll
  for (int i = 0; i < 32; ++i) a += ws[WS_Y + i * D + j];
#pragma unroll
  for (int i = 32; i < 68; ++i) bb += ws[WS_Y + i * D + j];
  out[j] = __float2bfloat16(mr(bf2f(x[j]) + mr(a)) + mr(bb));
}

extern "C" void kernel_launch(void* const* d_in, const int* in_sizes, int n_in, void* d_out, int out_size, void* d_ws, size_t ws_size, hipStream_t stream) {
  const unsigned short* x = (const unsigned short*)d_in[0];
  const unsigned short* K = (const unsigned short*)d_in[1];
  const unsigned short* V = (const unsigned short*)d_in[2];
  const unsigned short* wq = (const unsigned short*)d_in[3];
  const unsigned short* wo = (const unsigned short*)d_in[4];
  const unsigned short* wg = (const unsigned short*)d_in[6];
  const unsigned short* wu = (const unsigned short*)d_in[7];
  const unsigned short* wd = (const unsigned short*)d_in[8];
  float* ws = (float*)d_ws;
  k_pre<<<68, 256, 0, stream>>>(x, wq, wg, wu, ws);
  k_attn<<<256, AW * 64, 0, stream>>>(K, V, ws);
  k_post<<<68, 576, 0, stream>>>(wo, wd, ws);
  k_fin<<<9, 64, 0, stream>>>(x, ws, (__hip_bfloat16*)d_out);
}
```

```cpp
#include <hip/hip_runtime.h>
#include <hip/hip_bf16.h>
#include <cstdint>

constexpr int T_KV = 3550;
constexpr int T_KV2 = 3450;
constexpr int T_DELTA = 230;
constexpr int FFN_T0 = 40;
constexpr int FFN_DT = 22;
constexpr int EARLY_SLACK = 20;
constexpr int DRAIN = 30;
constexpr int KAPPA = 120;
constexpr int X1_WAIT = 130;
constexpr int X0_AT = 1050;
constexpr int XF_AT = 1300;
constexpr int XF_STAG = 30;
constexpr int X2_WAIT = 200;
constexpr int W_AT = 20;
constexpr int W_DT = 15;
constexpr int XO_WAIT = 45;
constexpr int X3_WAIT = 40;

#define LAS __attribute__((address_space(3)))
#define GAS __attribute__((address_space(1)))
typedef unsigned u32x4 __attribute__((ext_vector_type(4)));
typedef unsigned u32x2 __attribute__((ext_vector_type(2)));
typedef float f32x4 __attribute__((ext_vector_type(4)));
typedef float f32x2 __attribute__((ext_vector_type(2)));
typedef short s16x4 __attribute__((ext_vector_type(4)));
typedef __bf16 bf16x2 __attribute__((ext_vector_type(2)));
typedef __bf16 bf16x8 __attribute__((ext_vector_type(8)));

constexpr int D = 576, NSW = 8, NTHR = (NSW + 1) * 64;
constexpr int K0 = 12;
constexpr int NR = 72, RED0 = 256 - NR, RB = 256 * 64;
constexpr unsigned KV_BYTES = 32u * 16384u * 256u, WQ_BYTES = 576u * 8192u;
constexpr unsigned WS_EPOCH = 0, WS_X2 = 4096, WS_XT = 8192, WS_X1 = 49152, WS_XF = WS_X1 + 256 * 1024, WS_XO = WS_XF + 256 * 1024, WS_X3 = WS_XO + 256 * 1024,
                   WS_END = WS_X3 + NR * RB;
constexpr int L_W = 0, L_WS = 34 * 1056, L_XN = L_WS + 5 * 1024, L_OR = L_XN + 2304, L_GU = L_OR + 8 * 544, L_Q = L_GU + 4096, L_ML = L_Q + 256, L_FAC = L_ML + 128,
              L_HH = L_FAC + 32, L_OA = L_HH + 64, L_FLAG = L_OA + 32, L_RED = L_FLAG + 32, L_USED = L_RED + 8 * 32,
              L_ALLOC = 96 * 1024;
static_assert(L_USED <= L_ALLOC && L_RED % 16 == 0 && K0 % 4 == 0 && K0 >= 8 && K0 <= 16, "layout");
enum { F_TAG = 0, F_FAC = 1, F_CNT_F = 2, F_END = 3, F_BASE = 4, F_Q = 5, F_SHIFT = 6 };
constexpr int AUX_NT = 2, AUX_SC1 = 16;
constexpr unsigned SPIN_LIMIT = 400000;

__device__ __forceinline__ unsigned now() { return (unsigned)__builtin_amdgcn_s_memrealtime(); }
__device__ __forceinline__ void pace(unsigned due) {
  while ((int)(now() - due) < 0) __builtin_amdgcn_s_sleep(1);
  asm volatile("" ::: "memory");
}
__device__ __forceinline__ unsigned pk2(float a, float b) { const bf16x2 v = {(__bf16)a, (__bf16)b}; return __builtin_bit_cast(unsigned, v); }
__device__ __forceinline__ float hi16(unsigned u) { return __uint_as_float(u & 0xffff0000u); }
__device__ __forceinline__ float rbf(float a) { return hi16(pk2(a, a)); }
__device__ __forceinline__ float fl(unsigned u) { return __uint_as_float(u); }
__device__ __forceinline__ unsigned ui(float f) { return __float_as_uint(f); }
__device__ __forceinline__ float dot2(unsigned k, unsigned q, float acc) {
  return __builtin_amdgcn_fdot2_f32_bf16(__builtin_bit_cast(bf16x2, k), __builtin_bit_cast(bf16x2, q), acc, false);
}
template <int CTRL> __device__ __forceinline__ unsigned dppu(unsigned x) { return (unsigned)__builtin_amdgcn_mov_dpp((int)x, CTRL, 0xf, 0xf, true); }
template <int CTRL> __device__ __forceinline__ float dpp(float x) { return fl(dppu<CTRL>(ui(x))); }
constexpr int XOR1 = 0xB1, XOR2 = 0x4E, HMIR = 0x141, ROR4 = 0x124, ROR8 = 0x128, BCAST = 0x150;
__device__ __forceinline__ float swadd32(float a, float b) { auto s = __builtin_amdgcn_permlane32_swap(ui(a), ui(b), false, false); return fl(s[0]) + fl(s[1]); }
__device__ __forceinline__ float swadd16(float a, float b) { auto s = __builtin_amdgcn_permlane16_swap(ui(a), ui(b), false, false); return fl(s[0]) + fl(s[1]); }
__device__ __forceinline__ float swmax32(float a) { auto s = __builtin_amdgcn_permlane32_swap(ui(a), ui(a), false, false); return fmaxf(fl(s[0]), fl(s[1])); }
__device__ __forceinline__ float swmax16(float a) { auto s = __builtin_amdgcn_permlane16_swap(ui(a), ui(a), false, false); return fmaxf(fl(s[0]), fl(s[1])); }
__device__ __forceinline__ float sum8(float x) { x += dpp<XOR1>(x); x += dpp<XOR2>(x); x += dpp<HMIR>(x); return x; }
__device__ __forceinline__ float max8(float x) { x = fmaxf(x, dpp<XOR1>(x)); x = fmaxf(x, dpp<XOR2>(x)); x = fmaxf(x, dpp<HMIR>(x)); return x; }
__device__ __forceinline__ float sum64(float x) { x = sum8(x); x += dpp<ROR8>(x); x = swadd16(x, x); return swadd32(x, x); }
__device__ __forceinline__ float max64(float x) { x = max8(x); x = fmaxf(x, dpp<ROR8>(x)); return swmax32(swmax16(x)); }
__device__ __forceinline__ f32x4 mfma(u32x4 a, u32x4 b, f32x4 c) {
  return __builtin_amdgcn_mfma_f32_16x16x32_bf16(__builtin_bit_cast(bf16x8, a), __builtin_bit_cast(bf16x8, b), c, 0, 0, 0);
}
__device__ __forceinline__ f32x4 mfma16(u32x2 a, u32x2 b, f32x4 c) {
  return __builtin_amdgcn_mfma_f32_16x16x16bf16_1k(__builtin_bit_cast(s16x4, a), __builtin_bit_cast(s16x4, b), c, 0, 0, 0);
}
__device__ __forceinline__ __amdgpu_buffer_rsrc_t rsrc(const void* p, unsigned bytes) { return __builtin_amdgcn_make_buffer_rsrc(const_cast<void*>(p), 0, bytes, 0x00020000); }
#define LDS_WAIT() asm volatile("s_waitcnt lgkmcnt(0)" ::: "memory")
#define VM_WAIT(n) asm volatile("s_waitcnt vmcnt(%0)" :: "i"(n) : "memory")
#define BARRIER() do { LDS_WAIT(); __builtin_amdgcn_s_barrier(); asm volatile("" ::: "memory"); } while (0)

struct Args {
  const unsigned short *x, *k, *v, *wq, *wo, *wg, *wu, *wd;
  unsigned short* out;
  char* ws;
};

__device__ __forceinline__ void layernorm(const unsigned short (&xr)[9], LAS char* L, int lane) {
  float v[9], s = 0.f;
#pragma unroll
  for (int i = 0; i < 9; ++i) { v[i] = fl((unsigned)xr[i] << 16); s += v[i]; }
  const float mean = sum64(s) * (1.f / D);
  float q = 0.f;
#pragma unroll
  for (int i = 0; i < 9; ++i) { const float d = v[i] - mean; q = fmaf(d, d, q); }
  const float var = sum64(q) * (1.f / D);
  const float mb = rbf(mean), den = rbf(__builtin_amdgcn_sqrtf(rbf(rbf(var) + 1e-5f))), rd = __builtin_amdgcn_rcpf(den);
#pragma unroll
  for (int i = 0; i < 9; ++i) {
    const float n = rbf(v[i] - mb), r0 = n * rd, r = fmaf(fmaf(-r0, den, n), rd, r0);
    ((LAS unsigned*)(L + L_XN))[lane + 64 * i] = pk2(r, r);
  }
  LDS_WAIT();
}
__device__ __forceinline__ void dma1k(const char* src, unsigned voff, LAS char* dst) {
  unsigned keep;
  asm volatile("s_mov_b32 %0, m0\n\ts_mov_b32 m0, %3\n\ts_nop 0\n\tglobal_load_lds_dwordx4 %1, %2 nt\n\ts_mov_b32 m0, %0"
               : "=&s"(keep) : "v"(voff), "s"(src), "s"((unsigned)(uintptr_t)dst) : "memory");
}
__device__ __forceinline__ void wait_word(LAS char* L, int f, unsigned v) {
  for (const unsigned tw = now(); __builtin_amdgcn_readfirstlane(((volatile LAS unsigned*)(L + L_FLAG))[f]) != v && now() - tw < SPIN_LIMIT;) __builtin_amdgcn_s_sleep(1);
  asm volatile("" ::: "memory");
}
__device__ __forceinline__ bool none(bool p) { return __builtin_amdgcn_ballot_w64(p) == 0ull; }
#define PIN(x) asm volatile("" : "+v"(x))
__device__ __forceinline__ u32x2 tr4(LAS char* p) { return __builtin_bit_cast(u32x2, __builtin_amdgcn_ds_read_tr16_b64_v4i16((LAS s16x4*)p)); }
template <int DD> __device__ __forceinline__ f32x4 pv_tile(const u32x4 (&v)[4], unsigned P, const u32x4 M, f32x4 acc) {
  { const unsigned p = dppu<BCAST + 0 + DD>(P); acc = mfma(M & p, v[0], acc); }
  { const unsigned p = dppu<BCAST + 8 + DD>(P); acc = mfma(M & p, v[1], acc); }
  { const unsigned p = dppu<BCAST + 4 + DD>(P); acc = mfma(M & p, v[2], acc); }
  { const unsigned p = dppu<BCAST + 12 + DD>(P); acc = mfma(M & p, v[3], acc); }
  return acc;
}

__device__ __forceinline__ unsigned mean_time(u32x2 ta, unsigned tag, unsigned ref) {
  const float n = sum64(ta[1] == tag ? 1.f : 0.f), d = sum64(ta[1] == tag ? (float)(int)(ta[0] - ref) : 0.f);
  return ref + (int)fminf(fmaxf(d / fmaxf(n, 1.f), -300.f), 300.f);
}
__global__ void __launch_bounds__(NTHR, 3) fused_decode(Args a) {
  extern __shared__ __attribute__((aligned(16))) char lds_[];
  LAS char* L = (LAS char*)lds_;
  volatile LAS unsigned* flag = (volatile LAS unsigned*)(L + L_FLAG);
  if (threadIdx.x >= 64 && threadIdx.x < 72) { flag[threadIdx.x - 64] = 0u; ((LAS unsigned*)(L + L_ML))[4 * (threadIdx.x - 64) + 2] = 0u; }
  BARRIER();
  asm volatile("" :: "s"(a.x), "s"(a.ws));
  const unsigned t0 = now();
  const int lane = threadIdx.x & 63, wave = __builtin_amdgcn_readfirstlane(threadIdx.x >> 6), g = lane >> 4, c = lane & 15;
  const int b = blockIdx.x, jl = b >> 3, jj = (b & 7) * 32 + jl, h = jj >> 3, s = jj & 7, rb = b - RED0;
  const __amdgpu_buffer_rsrc_t rws = rsrc(a.ws, WS_END);
  constexpr float SC = 0.088388347648318440f, L2E = 1.4426950408889634f;
  const unsigned vo = lane * 16;
  u32x4 M;
#pragma unroll
  for (int e = 0; e < 4; ++e) M[e] = (c >> 1) == e ? ((c & 1) ? 0xffff0000u : 0x0000ffffu) : 0u;

  if (__builtin_expect(wave == 0, 1)) {
    const unsigned ep = __builtin_amdgcn_raw_buffer_load_b32(rws, 0, WS_EPOCH, AUX_SC1);
    unsigned short xr[9];
#pragma unroll
    for (int i = 0; i < 9; ++i) xr[i] = a.x[lane + 64 * i];
    __builtin_amdgcn_sched_barrier(0);
    const __amdgpu_buffer_rsrc_t rq = rsrc(a.wq, WQ_BYTES);
    u32x4 wr[18];
#pragma unroll
    for (int i = 0; i < 18; ++i) wr[i] = __builtin_amdgcn_raw_buffer_load_b128(rq, g * 8192 + c * 16, (72 * s + 4 * i) * 8192 + h * 256, AUX_NT);
    __builtin_amdgcn_sched_barrier(0);
    layernorm(xr, L, lane);
    const unsigned tag = __builtin_amdgcn_readfirstlane(ep) + 1u;
    const bool sample = lane < 8 && (jj & 3) == 0;
    if (sample) __builtin_amdgcn_raw_buffer_store_b64(u32x2{t0, tag}, rws, lane * 4608, WS_XT + jj * 2, AUX_SC1);
    f32x4 acc = {0.f, 0.f, 0.f, 0.f};
#pragma unroll
    for (int i = 0; i < 18; ++i) acc = mfma(M & ((LAS unsigned*)(L + L_XN))[72 * s + 4 * i + g], wr[i], acc);
    if (lane < 32) *(LAS f32x4*)(L + L_OR + c * 32 + g * 16) = acc;
    LDS_WAIT();
    { const f32x2 o = *(LAS f32x2*)(L + L_OR + lane * 8); __builtin_amdgcn_raw_buffer_store_b128(u32x4{ui(o[0]), tag, ui(o[1]), tag}, rws, vo, WS_X1 + jj * 1024, AUX_SC1); }
    const unsigned tp = now();
    pace(tp + X1_WAIT);
    u32x2 ta = __builtin_amdgcn_raw_buffer_load_b64(rws, lane * 8, WS_XT + (b & 7) * 4608, AUX_SC1);
    u32x4 r[8];
    for (;;) {
      asm volatile("" ::: "memory");
#pragma unroll
      for (int p = 0; p < 8; ++p) r[p] = __builtin_amdgcn_raw_buffer_load_b128(rws, vo + (p & 3) * 1024, WS_X1 + (8 * h + (p & 4)) * 1024, AUX_SC1);
      unsigned bad = 0;
#pragma unroll
      for (int p = 0; p < 8; ++p) bad |= (r[p][1] ^ tag) | (r[p][3] ^ tag);
      if (none(bad != 0u) || now() - tp > SPIN_LIMIT) break;
    }
    float q0 = 0.f, q1 = 0.f;
#pragma unroll
    for (int p = 0; p < 8; ++p) { q0 += fl(r[p][0]); q1 += fl(r[p][2]); }
    ((LAS unsigned*)(L + L_Q))[lane] = pk2(q0, q1);
    const unsigned tm = __builtin_amdgcn_readfirstlane(mean_time(ta, tag, t0));
    unsigned sh = __builtin_amdgcn_readfirstlane(flag[F_SHIFT]);
    { const int late = (int)(now() + 25 - (t0 + T_DELTA + sh + T_KV / 8)); sh += late > 0 ? late : 0; sh += sh > 20u ? DRAIN : 0; }
    const unsigned t1 = (int)(tm - (t0 + sh)) > 0 ? tm : t0 + sh;
    if (lane == 0) {
      __hip_atomic_fetch_max((LAS unsigned*)(L + L_FLAG) + F_SHIFT, sh, __ATOMIC_RELAXED, __HIP_MEMORY_SCOPE_WORKGROUP);
      flag[F_TAG] = tag; flag[F_BASE] = t1; flag[F_END] = t1;
    }
    LDS_WAIT();
    if (lane == 0) flag[F_Q] = 1u;
    if (sample) __builtin_amdgcn_raw_buffer_store_b64(u32x2{t1, tag}, rws, lane * 4608, WS_XT + 512 + jj * 2, AUX_SC1);
    wait_word(L, F_CNT_F, NSW);
    {
      f32x2 o = {0.f, 0.f};
#pragma unroll
      for (int i = 0; i < 8; ++i) o += *(LAS f32x2*)(L + L_GU + i * 512 + lane * 8);
      __builtin_amdgcn_raw_buffer_store_b128(u32x4{ui(o[0]), tag, ui(o[1]), tag}, rws, vo, WS_XF + jj * 1024, AUX_SC1);
    }
    pace(t1 + X0_AT + (jl & 7) * 15);
    for (const unsigned tw = now();;) {
      ta = __builtin_amdgcn_raw_buffer_load_b64(rws, lane * 8, WS_XT + 512 + (b & 7) * 4608, AUX_SC1);
      if (none(ta[1] != tag) || now() - tw > SPIN_LIMIT) break;
      pace(now() + 100);
    }
    {
      const unsigned te = mean_time(ta, tag, t1);
      const float far = sum64(fabsf((float)(int)(ta[0] - te))) * (KAPPA / 6400.f);
      if (lane == 0) flag[F_END] = te + (int)fminf(far, 150.f);
    }
    if (rb >= 0 && lane == 0) __builtin_amdgcn_raw_buffer_store_b128(u32x4{0u, 0u, 0u, 0u}, rsrc(a.out, D * 2), 0, rb * 16, AUX_SC1);
    pace(t1 + XF_AT - 100 + jl * XF_STAG);
    { const unsigned tv = __builtin_nontemporal_load((const GAS unsigned*)(lane == 0 ? a.wd + jj * 10368 : lane == 1 ? a.wo + jj * 9216 : a.v + (size_t)jj * 262144)); asm volatile("" :: "v"(tv)); }
    pace(t1 + XF_AT + jl * XF_STAG);
    const int n = 18 * jl + 2 * (c < 9 ? c : 8), nb = n >> 6;
    const bool used = nb < 5 || g < 3;
    const unsigned fo = (nb < 5 ? 4 * nb + g : 20 + 3 * (nb - 5) + (g < 3 ? g : 0)) * 1024 + ((n & 63) >> 1) * 16;
    u32x4 xg, xu;
    for (const unsigned tw = now();;) {
      xg = __builtin_amdgcn_raw_buffer_load_b128(rws, fo, WS_XF + (b & 7) * 32768, AUX_SC1);
      xu = __builtin_amdgcn_raw_buffer_load_b128(rws, fo + 512, WS_XF + (b & 7) * 32768, AUX_SC1);
      if (none(xg[1] != tag || xg[3] != tag || xu[1] != tag || xu[3] != tag) || now() - tw > SPIN_LIMIT) break;
      pace(now() + 100);
    }
    float gu[4] = {used ? fl(xg[0]) : 0.f, used ? fl(xg[2]) : 0.f, used ? fl(xu[0]) : 0.f, used ? fl(xu[2]) : 0.f};
#pragma unroll
    for (int e = 0; e < 4; ++e) { gu[e] = swadd16(gu[e], gu[e]); gu[e] = rbf(swadd32(gu[e], gu[e])); }
    if (lane < 16) ((LAS unsigned*)(L + L_HH))[lane] = c < 9 ? pk2(gu[0] * gu[2], gu[1] * gu[3]) : 0u;
    u32x4 ml;
    for (const unsigned tw = now();;) {
      ml = *(volatile LAS u32x4*)(L + L_ML + (lane & 7) * 16);
      if (none(ml[2] == 0u) || now() - tw > SPIN_LIMIT) break;
      __builtin_amdgcn_s_sleep(8);
    }
    const float mw = fl(ml[0]), lw = fl(ml[1]);
    const float mg = max8(mw), lg = sum8(lw * __builtin_amdgcn_exp2f((mw - mg) * L2E));
    if (lane == 0) __builtin_amdgcn_raw_buffer_store_b128(u32x4{ui(mg), tag, ui(lg), tag}, rws, 0, WS_X2 + jj * 16, AUX_SC1);
    u32x4 x2;
    for (const unsigned tw = now();;) {
      pace(now() + X2_WAIT);
      x2 = __builtin_amdgcn_raw_buffer_load_b128(rws, (lane & 7) * 16, WS_X2 + h * 128, AUX_SC1);
      if (none(x2[1] != tag || x2[3] != tag) || now() - tw > SPIN_LIMIT) break;
    }
    const float ms = fl(x2[0]), ls = fl(x2[2]);
    const float mh = max8(ms), lh = sum8(ls * __builtin_amdgcn_exp2f((ms - mh) * L2E));
    if (lane < 8) ((LAS float*)(L + L_FAC))[lane] = __builtin_amdgcn_exp2f((mw - mh) * L2E) / lh;
    LDS_WAIT();
    if (lane == 0) flag[F_FAC] = 1u;
    return;
  }

  const int w = wave - 1;
  const int lb = jl < 20 ? jl >> 2 : 5 + (jl - 20) / 3, part = jl < 20 ? jl & 3 : (jl - 20) % 3, nld = jl < 20 ? 36 : 48;
  u32x4 fr[6];
  {
    const GAS char* src = (const GAS char*)(c < 8 ? a.wg : a.wu) + (size_t)(4 * (nld * part + w) + g) * 9216 + ((b & 7) * 9 + lb) * 128 + (c & 7) * 16;
    pace(t0 + FFN_T0 + w * FFN_DT);
#pragma unroll
    for (int j = 0; j < 6; ++j) fr[j] = __builtin_nontemporal_load((const GAS u32x4*)(src + (size_t)(j < 4 || w + 8 * j < nld ? j : 0) * 32 * 9216));
    __builtin_amdgcn_sched_barrier(0);
  }
  const unsigned base = jj * 524288 + (w & 3) * 2048 + (w >> 2) * 16384;
  unsigned tb = t0 + T_DELTA + ((w * T_KV) >> 8), tq = 0, step = 8 * T_KV, tend = 0;
  u32x4 ring[4][4];
  auto issue = [&](__amdgpu_buffer_rsrc_t r, int k, int d, bool early) {
    const unsigned sh = early ? __builtin_amdgcn_readfirstlane(flag[F_SHIFT]) : 0u, due = tb + sh + (tq >> 8);
    pace(due);
    VM_WAIT(0);
    tq += step;
#pragma unroll
    for (int u = 0; u < 4; ++u) ring[d][u] = __builtin_amdgcn_raw_buffer_load_b128(r, vo + (u & 1) * 1024 + (u >> 1) * 8192, base + (k & 15) * 32768, AUX_NT);
    __builtin_amdgcn_sched_barrier(0);
    if (early) {
      const int l = (int)(now() - due) - EARLY_SLACK;
      if (l > 0 && lane == 0) __hip_atomic_fetch_max((LAS unsigned*)(L + L_FLAG) + F_SHIFT, sh + l, __ATOMIC_RELAXED, __HIP_MEMORY_SCOPE_WORKGROUP);
    }
  };
#pragma unroll
  for (int k = 0; k < 4; ++k) issue(rsrc(a.k, KV_BYTES), k, k, true);
  const unsigned xres = a.x[(rb >= 0 ? rb : 0) * 8 + (c & 7)];
  wait_word(L, F_Q, 1u);
  {
    const unsigned sh = __builtin_amdgcn_readfirstlane(flag[F_SHIFT]);
    const int drift = (int)(__builtin_amdgcn_readfirstlane(flag[F_BASE]) - (t0 + sh));
    tb += sh + (tq >> 8); tq = 0;
    step += ((drift > -300 ? drift : -300) * (65536 / (K0 - 4))) >> 8;
  }
  {
    f32x4 gu = {0.f, 0.f, 0.f, 0.f};
#pragma unroll
    for (int j = 0; j < 6; ++j) {
      const bool in = j < 4 || w + 8 * j < nld;
      const unsigned xv = ((LAS unsigned*)(L + L_XN))[4 * (nld * part + w + (in ? 8 * j : 0)) + g];
      gu = mfma(M & (in ? xv : 0u), fr[j], gu);
    }
    if (lane < 32) *(LAS f32x4*)(L + L_GU + w * 512 + c * 32 + g * 16) = gu;
    LDS_WAIT();
    if (lane == 0) __hip_atomic_fetch_add((LAS unsigned*)(L + L_FLAG) + F_CNT_F, 1u, __ATOMIC_RELAXED, __HIP_MEMORY_SCOPE_WORKGROUP);
  }
  const u32x4 q = *(LAS u32x4*)(L + L_Q + c * 16);
  const unsigned tag = __builtin_amdgcn_readfirstlane(flag[F_TAG]);
  const bool selA = (c & 8) != 0, selB = (c & 4) != 0;
  float S[4] = {0.f, 0.f, 0.f, 0.f};
#pragma unroll 1
  for (int k0 = 4; k0 < 20; k0 += 4) {
    if (k0 == K0) {
      const unsigned n = tb + (tq >> 8), t = now();
      tb = (int)(t - n) > 0 ? t : n; tq = 0;
      tend = __builtin_amdgcn_readfirstlane(flag[F_END]) + T_DELTA + (K0 * T_KV + (32 - K0) * T_KV2) / 32;
      const int sp = (int)(tend - (((8 - w) * T_KV2) >> 8) - tb);
      step = sp > 0 ? (sp * (65536 / (31 - K0))) >> 8 : 0;
    }
    const __amdgpu_buffer_rsrc_t r = rsrc(k0 < 16 ? a.k : a.v, KV_BYTES);
    float keep = 0.f;
#pragma unroll
    for (int d = 0; d < 4; ++d) {
      float x[4];
#pragma unroll
      for (int u = 0; u < 4; ++u) {
        float t = 0.f;
#pragma unroll
        for (int e = 0; e < 4; ++e) t = dot2(ring[d][u][e], q[e], t);
        x[u] = t;
      }
      const float y0 = (selA ? x[1] : x[0]) + dpp<ROR8>(selA ? x[0] : x[1]);
      const float y1 = (selA ? x[3] : x[2]) + dpp<ROR8>(selA ? x[2] : x[3]);
      float z = (selB ? y1 : y0) + dpp<HMIR>(selB ? y0 : y1);
      z += dpp<XOR1>(z); z += dpp<XOR2>(z);
      keep = (c & 3) == d ? z : keep;
      PIN(keep);
      issue(r, k0 + d, d, false);
    }
    S[0] = S[1]; S[1] = S[2]; S[2] = S[3]; S[3] = keep;
  }
  unsigned P[4];
  {
    float m = -INFINITY;
#pragma unroll
    for (int r = 0; r < 4; ++r) { S[r] = rbf(rbf(S[r]) * SC); m = fmaxf(m, S[r]); }
    m = max64(m);
    float l = 0.f;
#pragma unroll
    for (int r = 0; r < 4; ++r) { const float p = __builtin_amdgcn_exp2f((S[r] - m) * L2E); l += p; P[r] = pk2(p, p); }
    l = sum64(l);
    if (lane == 0) *(volatile LAS u32x4*)(L + L_ML + w * 16) = u32x4{ui(m), ui(l), 1u, 0u};
  }
  f32x4 acc = {0.f, 0.f, 0.f, 0.f};
  const __amdgpu_buffer_rsrc_t rv = rsrc(a.v, KV_BYTES);
#pragma unroll 1
  for (int k0 = 20; k0 < 32; k0 += 4) {
    acc = pv_tile<0>(ring[0], P[0], M, acc); PIN(acc); issue(rv, k0, 0, false);
    acc = pv_tile<1>(ring[1], P[0], M, acc); PIN(acc); issue(rv, k0 + 1, 1, false);
    acc = pv_tile<2>(ring[2], P[0], M, acc); PIN(acc); issue(rv, k0 + 2, 2, false);
    acc = pv_tile<3>(ring[3], P[0], M, acc); PIN(acc); issue(rv, k0 + 3, 3, false);
    P[0] = P[1]; P[1] = P[2]; P[2] = P[3];
  }
  acc = pv_tile<0>(ring[0], P[0], M, acc);
  acc = pv_tile<1>(ring[1], P[0], M, acc);
  acc = pv_tile<2>(ring[2], P[0], M, acc); PIN(acc);
  wait_word(L, F_FAC, 1u);
  const float fac = ((LAS float*)(L + L_FAC))[w];
  u32x4 AH = {((LAS unsigned*)(L + L_HH))[2 * g], ((LAS unsigned*)(L + L_HH))[2 * g + 1], 0u, ((LAS unsigned*)(L + L_HH))[g == 3 ? 8 : 9]};
  const int wr = 4 * g + (c >> 2), wch = 2 * (w & 3) + ((c & 3) >> 1);
  unsigned aw = L_OR + w * 544 + c * 32 + g * 16, ar = L_OR + (lane & 7) * 544 + w * 64 + (lane >> 3) * 8, ao = L_OA + g * 8;
  unsigned am = L_W + wr * 1056 + w * 128 + (c & 3) * 8;
  unsigned as1 = L_WS + (c & 1) * 8 + wr * 128 + ((wch ^ (wr & 6)) * 16), as2 = L_WS + (c & 1) * 8 + (wr + 18) * 128 + ((wch ^ ((wr + 18) & 6)) * 16);
  unsigned sv = (w == 2 && (lane >> 3) > 1 ? 1 : lane >> 3) * 1152 + 1024 + ((lane & 7) ^ ((lane >> 3) & 6)) * 16;
  unsigned po = (lane >> 3) * 16, oo = (c & 7) * 1024 + (2 * g + (c >> 3)) * 16, yo = g * 32768 + (c >> 3) * RB + (c & 7) * 8;
  unsigned ae = L_RED + ((2 * (lane & 31) + (lane >> 5)) & 7) * 32 + w * 4, af = L_RED + (c & 7) * 32;
  unsigned m0 = g == 0 ? ~0u : 0u, m1 = g == 1 ? ~0u : 0u, m2 = g == 2 ? ~0u : 0u, m3 = g == 3 ? ~0u : 0u;
  unsigned so = WS_XO + jj * 1024 + w * 128, sy4 = WS_X3 + 4 * w * 32768 + jj * 64, sy1 = WS_X3 + (32 + w) * 32768 + jj * 64;
  const unsigned ob = WS_XO + h * 8192 + s * 128;
  PIN(AH); PIN(aw); PIN(ar); PIN(ao); PIN(am); PIN(as1); PIN(as2); PIN(sv); PIN(po); PIN(oo); PIN(yo); PIN(ae); PIN(af); PIN(m0); PIN(m1); PIN(m2); PIN(m3);
  asm volatile("" : "+s"(so), "+s"(sy4), "+s"(sy1));
  LDS_WAIT();
  __builtin_amdgcn_sched_barrier(0);
  acc = pv_tile<3>(ring[3], P[0], M, acc);
  if (lane < 32) *(LAS f32x4*)(L + aw) = acc * fac;
  auto load_w = [&](const char* src, int r0, int nr, int sp) {
#pragma unroll
    for (int j = 0; j < nr; ++j) dma1k(src + (r0 + j) * 1152, vo, L + L_W + (r0 + j) * 1056);
    dma1k(src + sp * 8 * 1152, sv, L + L_WS + sp * 1024);
  };
  if (w < 3) { pace(tend + W_AT + w * W_DT); load_w((const char*)a.wd + (size_t)jj * 20736 - 16 * 1152, 16 + 6 * w, 6, 2 + w); }
  BARRIER();
  {
    const f32x2 o = *(LAS f32x2*)(L + ar);
    const float o0 = sum8(o[0]), o1 = sum8(o[1]);
    if ((lane & 7) == 0) __builtin_amdgcn_raw_buffer_store_b128(u32x4{ui(o0), tag, ui(o1), tag}, rws, po, so, AUX_SC1);
  }
  const unsigned tpub = now();
  u32x4 xo = {0u, 0u, 0u, 0u};
  if (w == 7) { pace(tpub + XO_WAIT); xo = __builtin_amdgcn_raw_buffer_load_b128(rws, oo, ob, AUX_SC1); }
  else if (w == 3 || w == 4) { if (w == 4) pace(tpub + XO_WAIT + 3); load_w((const char*)a.wo + (size_t)jj * 18432, 8 * (w - 3), 8, w - 3); }
  else if (w < 3) VM_WAIT(1);
  __builtin_amdgcn_s_barrier();
  asm volatile("" ::: "memory");
  f32x4 d[5];
  {
    u32x2 B1[5], B2[5];
#pragma unroll
    for (int n = 0; n < 4; ++n) { B1[n] = tr4(L + am + 16 * 1056 + n * 32); B2[n] = tr4(L + am + 18 * 1056 + n * 32); }
    B1[4] = tr4(L + as1 + 16 * 128); B2[4] = tr4(L + as2);
#pragma unroll
    for (int n = 0; n < 5; ++n) { d[n] = mfma(AH, u32x4{B1[n][0], B1[n][1], B2[n][0], B2[n][1]}, f32x4{0.f, 0.f, 0.f, 0.f}); PIN(d[n]); }
  }
  if (w == 7) {
    while (!none(xo[1] != tag || xo[3] != tag) && now() - tpub < SPIN_LIMIT) {
      asm volatile("" ::: "memory");
      xo = __builtin_amdgcn_raw_buffer_load_b128(rws, oo, ob, AUX_SC1);
    }
    const unsigned pk = pk2(sum8(fl(xo[0])), sum8(fl(xo[2])));
    if ((c & 7) == 0) *(LAS unsigned*)(L + ao + (c >> 1)) = pk;
  }
  VM_WAIT(0);
  BARRIER();
  {
    const u32x2 A0 = *(LAS u32x2*)(L + ao);
    u32x2 B0[5];
#pragma unroll
    for (int n = 0; n < 4; ++n) B0[n] = tr4(L + am + n * 32);
    B0[4] = tr4(L + as1);
    __builtin_amdgcn_sched_barrier(0);
#pragma unroll
    for (int n = 0; n < 5; ++n) d[n] = mfma16(A0, B0[n], d[n]);
    __builtin_amdgcn_sched_barrier(0);
    const unsigned y4 = (ui(d[0][0]) & m0) | (ui(d[1][0]) & m1) | (ui(d[2][0]) & m2) | (ui(d[3][0]) & m3);
    __builtin_amdgcn_raw_buffer_store_b64(u32x2{y4, tag}, rws, yo, sy4, AUX_SC1);
    if (w < 4 && lane < 16) __builtin_amdgcn_raw_buffer_store_b64(u32x2{ui(d[4][0]), tag}, rws, yo, sy1, AUX_SC1);
  }
  if (rb < 0) return;
  u32x4 r0, r1;
  const unsigned tp = now();
  pace(tp + X3_WAIT);
  for (;;) {
    asm volatile("" ::: "memory");
    r0 = __builtin_amdgcn_raw_buffer_load_b128(rws, vo, WS_X3 + rb * RB + w * 2048, AUX_SC1);
    r1 = __builtin_amdgcn_raw_buffer_load_b128(rws, vo + 1024, WS_X3 + rb * RB + w * 2048, AUX_SC1);
    if (none(((r0[1] ^ tag) | (r0[3] ^ tag) | (r1[1] ^ tag) | (r1[3] ^ tag)) != 0u) || now() - tp > SPIN_LIMIT) break;
  }
  float t = swadd32(fl(r0[0]) + fl(r1[0]), fl(r0[2]) + fl(r1[2]));
  t = swadd16(t, t);
  t += dpp<ROR8>(t);
  t += dpp<ROR4>(t);
  if ((lane & 31) < 4) *(LAS float*)(L + ae) = t;
  BARRIER();
  if (w == 0) {
    const f32x4 u = *(LAS f32x4*)(L + af) + *(LAS f32x4*)(L + af + 16);
    const float ov = ((u[0] + u[1]) + (u[2] + u[3])) + fl(xres << 16);
    const unsigned p2 = pk2(ov, dpp<XOR1>(ov));
    const u32x4 res = {p2, dppu<BCAST + 2>(p2), dppu<BCAST + 4>(p2), dppu<BCAST + 6>(p2)};
    if (lane == 0) {
      __builtin_amdgcn_raw_buffer_store_b128(res, rsrc(a.out, D * 2), 0, rb * 16, AUX_SC1);
      if (rb == 0) __builtin_amdgcn_raw_buffer_store_b32(tag, rws, 0, WS_EPOCH, AUX_SC1);
    }
  }

}

extern "C" void kernel_launch(void* const* d_in, const int* in_sizes, int n_in, void* d_out, int out_size, void* d_ws, size_t ws_size, hipStream_t stream) {
  Args a;
  a.x = (const unsigned short*)d_in[0]; a.k = (const unsigned short*)d_in[1]; a.v = (const unsigned short*)d_in[2];
  a.wq = (const unsigned short*)d_in[3]; a.wo = (const unsigned short*)d_in[4];
  a.wg = (const unsigned short*)d_in[6]; a.wu = (const unsigned short*)d_in[7]; a.wd = (const unsigned short*)d_in[8];
  a.out = (unsigned short*)d_out; a.ws = (char*)d_ws;
  (void)hipFuncSetAttribute((const void*)fused_decode, hipFuncAttributeMaxDynamicSharedMemorySize, L_ALLOC);
  fused_decode<<<256, NTHR, L_ALLOC, stream>>>(a);
}
```
